```python
import torch

def _softmax(x, axis=-1):
    return torch.softmax(x, dim=axis)
def _gelu(x):
    return torch.nn.functional.gelu(x, approximate="tanh")
def _silu(x):
    return torch.nn.functional.silu(x)
def _sigmoid(x):
    return torch.sigmoid(x)
def _layernorm(x, axis=-1, eps=1e-5):
    m = x.mean(dim=axis, keepdim=True)
    v = x.var(dim=axis, keepdim=True, unbiased=False)
    return (x - m) / torch.sqrt(v + eps)
def _scatter_add(tgt, idx, src, axis=0):
    return tgt.clone().index_add_(axis, idx, src)
def _segment_sum(src, seg, n):
    out = torch.zeros((n,) + src.shape[1:], dtype=src.dtype)
    return out.index_add_(0, seg, src)
def _pad(x, pw):
    flat = tuple(v for pair in reversed(pw) for v in pair)
    return torch.nn.functional.pad(x, flat)

kernel_name = 'concat_heads__mha_block_cached__mha_block_cached__near_ridge'

def setup_inputs():

    _w = (lambda a: (lambda t: t.to(torch.bfloat16) if t.is_floating_point() else t)(torch.as_tensor(a)))
    def _A_setup():
        B, Sq, D = 8, 144, 448
        H, Sk, Dh = 16, 512, 256
        return {
            "x": _w((lambda *s: torch.randn(*s, dtype=torch.bfloat16) * 0.8)(B, Sq, D)),
            "k_cache": _w((lambda *s: torch.randn(*s, dtype=torch.bfloat16) * 0.8)(B, H, Sk, Dh)),
            "v_cache": _w((lambda *s: torch.randn(*s, dtype=torch.bfloat16) * 0.05)(B, H, Sk, Dh)),
            "wq": _w((lambda *s: torch.randn(*s, dtype=torch.bfloat16) * 0.8)(D, H * Dh) * (D ** -0.5)),
            "wo": _w((lambda *s: torch.randn(*s, dtype=torch.bfloat16) * 0.05)(H * Dh, D) * ((H * Dh) ** -0.5)),
        }
    _A_in = _A_setup()

    def _B_setup():
        B, Sq, D = 8, 144, 448
        H, Sk, Dh = 16, 512, 256
        return {
            "x": _w((lambda *s: torch.randn(*s, dtype=torch.bfloat16) * 0.8)(B, Sq, D)),
            "k_cache": _w((lambda *s: torch.randn(*s, dtype=torch.bfloat16) * 0.8)(B, H, Sk, Dh)),
            "v_cache": _w((lambda *s: torch.randn(*s, dtype=torch.bfloat16) * 0.05)(B, H, Sk, Dh)),
            "wq": _w((lambda *s: torch.randn(*s, dtype=torch.bfloat16) * 0.8)(D, H * Dh) * (D ** -0.5)),
            "wo": _w((lambda *s: torch.randn(*s, dtype=torch.bfloat16) * 0.05)(H * Dh, D) * ((H * Dh) ** -0.5)),
        }
    _B_in = _B_setup()

    return {
        "A_x": _A_in["x"],
        "A_k_cache": _A_in["k_cache"],
        "A_v_cache": _A_in["v_cache"],
        "A_wq": _A_in["wq"],
        "A_wo": _A_in["wo"],
        "B_x": _B_in["x"],
        "B_k_cache": _B_in["k_cache"],
        "B_v_cache": _B_in["v_cache"],
        "B_wq": _B_in["wq"],
        "B_wo": _B_in["wo"],
    }

def reference(A_x, A_k_cache, A_v_cache, A_wq, A_wo, B_x, B_k_cache, B_v_cache, B_wq, B_wo):
    def _A_ref(x, k_cache, v_cache, wq, wo):
        B, Sq, D = x.shape
        H, Dh = 16, 256
        q = (lambda x, *d: x.permute(*d))((x @ wq).reshape(B, Sq, H, Dh), 0, 2, 1, 3)
        scores = torch.einsum("bhqd,bhkd->bhqk", q, k_cache) * (Dh ** -0.5)
        p = _softmax(scores, axis=-1)
        o = torch.einsum("bhqk,bhkd->bhqd", p, v_cache)
        return (lambda x, *d: x.permute(*d))(o, 0, 2, 1, 3).reshape(B, Sq, H * Dh) @ wo

    def _B_ref(x, k_cache, v_cache, wq, wo):
        B, Sq, D = x.shape
        H, Dh = 16, 256
        q = (lambda x, *d: x.permute(*d))((x @ wq).reshape(B, Sq, H, Dh), 0, 2, 1, 3)
        scores = torch.einsum("bhqd,bhkd->bhqk", q, k_cache) * (Dh ** -0.5)
        p = _softmax(scores, axis=-1)
        o = torch.einsum("bhqk,bhkd->bhqd", p, v_cache)
        return (lambda x, *d: x.permute(*d))(o, 0, 2, 1, 3).reshape(B, Sq, H * Dh) @ wo

    a = _A_ref(A_x, A_k_cache, A_v_cache, A_wq, A_wo)
    b = _B_ref(B_x, B_k_cache, B_v_cache, B_wq, B_wo)
    return torch.concatenate([a, b], axis=-1)
```

```cpp
#include <hip/hip_runtime.h>
#include <hip/hip_bf16.h>
#include <stdint.h>
#include <type_traits>

typedef __attribute__((ext_vector_type(8))) short bf16x8;
typedef __attribute__((ext_vector_type(4))) short s16x4;
typedef __attribute__((ext_vector_type(4))) float f32x4;
typedef __attribute__((ext_vector_type(4))) unsigned u32x4;
typedef __attribute__((ext_vector_type(2))) unsigned u32x2;
typedef __hip_bfloat16 bf16;

constexpr int NB = 8, NH = 16, SQ = 144, SK = 512, DH = 256, DM = 448, HD = NH * DH;
constexpr int NCW = 9, NLW = 2, NW = NCW + NLW, NTHR = NW * 64;
constexpr int TILE = 32768, RING = 4;
constexpr int NSTEP = 30;
constexpr int LDS_BYTES = RING * TILE;
constexpr int IPT = 32 / NLW;
constexpr size_t WS_CTR_BYTES = 16 * 256;
constexpr size_t PART_UNIT = (size_t)SQ * DM;

struct Params {
  const bf16* x[2];
  const bf16* k[2];
  const bf16* v[2];
  const bf16* wq[2];
  const bf16* wo[2];
  bf16* out;
  char* ws;
};

__device__ __forceinline__ unsigned cvtpk(float lo, float hi) {
  unsigned r;
  asm volatile("v_cvt_pk_bf16_f32 %0, %1, %2" : "=v"(r) : "v"(lo), "v"(hi));
  return r;
}
__device__ __forceinline__ bf16x8 pack8(const f32x4& a, const f32x4& b) {
  u32x4 w = {cvtpk(a[0], a[1]), cvtpk(a[2], a[3]), cvtpk(b[0], b[1]), cvtpk(b[2], b[3])};
  return __builtin_bit_cast(bf16x8, w);
}
typedef __attribute__((address_space(3))) s16x4 lds_s16x4;
__device__ __forceinline__ s16x4 lds_tr(unsigned a) {
  return __builtin_amdgcn_ds_read_tr16_b64_v4i16((lds_s16x4*)(uintptr_t)a);
}
__device__ __forceinline__ s16x4 lds_rd64(unsigned a) {
  return *(lds_s16x4*)(uintptr_t)a;
}
__device__ __forceinline__ bf16x8 join(s16x4 a, s16x4 b) {
  return (bf16x8){a[0], a[1], a[2], a[3], b[0], b[1], b[2], b[3]};
}
__device__ __forceinline__ void wg_barrier() {
  asm volatile("s_waitcnt lgkmcnt(0)\n\ts_barrier" ::: "memory");
}

template <int I, int N, class F>
__device__ __forceinline__ void static_for(F&& f) {
  if constexpr (I < N) { f(std::integral_constant<int, I>{}); static_for<I + 1, N>(f); }
}
template <int KIND> struct Cfg;
template <> struct Cfg<0> {
  static constexpr bool TR = true;
  static constexpr int ia(int i) { return i >> 2; }
  static constexpr int olo(int i) { return ((i >> 1) & 1) * 256 + (i & 1) * 32 * 512; }
  static constexpr int ohi(int i) { return olo(i) + 4 * 512; }
  static constexpr int iacc(int i) { return (i >> 2) + 8 * ((i >> 1) & 1); }
  static constexpr int ib(int i) { return i & 1; }
};
template <> struct Cfg<1> {
  static constexpr bool TR = false;
  static constexpr int ia(int i) { return 2 * (i >> 3); }
  static constexpr int olo(int i) { return (i & 3) * 8192 + ((i >> 2) & 1) * 256; }
  static constexpr int ohi(int i) { return olo(i); }
  static constexpr int iacc(int i) { return i & 3; }
  static constexpr int ib(int i) { return (i >> 3) + 4 * ((i >> 2) & 1); }
};
template <> struct Cfg<2> {
  static constexpr bool TR = true;
  static constexpr int ia(int i) { return i >> 2; }
  static constexpr int olo(int i) { return ((i >> 1) & 1) * 256 + (i & 1) * 32 * 512; }
  static constexpr int ohi(int i) { return olo(i) + 16 * 512; }
  static constexpr int iacc(int i) { return (i >> 2) + 8 * ((i >> 1) & 1); }
  static constexpr int ib(int i) { return i & 1; }
};
template <> struct Cfg<3> {
  static constexpr bool TR = true;
  static constexpr int ia(int i) { return i >> 3; }
  static constexpr int olo(int i) { return (i & 7) * 4096; }
  static constexpr int ohi(int i) { return olo(i) + 2048; }
  static constexpr int iacc(int i) { return i >> 3; }
  static constexpr int ib(int i) { return i & 7; }
};

#define PDEPTH 3
constexpr int PD = PDEPTH;
template <int KIND, int I>
__device__ __forceinline__ void frag_issue(const unsigned* addr, s16x4* lo, s16x4* hi) {
  using C = Cfg<KIND>;
  s16x4 a, b;
  if constexpr (C::TR) {
    asm volatile("ds_read_b64_tr_b16 %0, %1 offset:%2" : "=v"(a) : "v"(addr[C::ia(I)]), "i"(C::olo(I)));
    asm volatile("ds_read_b64_tr_b16 %0, %1 offset:%2" : "=v"(b) : "v"(addr[C::ia(I)]), "i"(C::ohi(I)));
  } else {
    asm volatile("ds_read_b64 %0, %1 offset:%2" : "=v"(a) : "v"(addr[C::ia(I)]), "i"(C::olo(I)));
    asm volatile("ds_read_b64 %0, %1 offset:%2" : "=v"(b) : "v"(addr[C::ia(I) + 1]), "i"(C::ohi(I)));
  }
  lo[I] = a; hi[I] = b;
}
template <int KIND, int I>
__device__ __forceinline__ void gemm_step_rec(const unsigned* addr, f32x4* acc, const bf16x8* bfr,
                                              s16x4* lo, s16x4* hi) {
  using C = Cfg<KIND>;
  if constexpr (I < 32) {
    if constexpr (I + PD < 32) frag_issue<KIND, I + PD>(addr, lo, hi);
    constexpr int pend = 2 * ((I + PD < 32) ? PD : (31 - I));
    s16x4 a = lo[I], b = hi[I];
    asm volatile("s_waitcnt lgkmcnt(%2)" : "+v"(a), "+v"(b) : "i"(pend));
    acc[C::iacc(I)] = __builtin_amdgcn_mfma_f32_16x16x32_bf16(join(a, b), bfr[C::ib(I)], acc[C::iacc(I)], 0, 0, 0);
    __builtin_amdgcn_sched_barrier(0);
    gemm_step_rec<KIND, I + 1>(addr, acc, bfr, lo, hi);
  }
}
template <int KIND>
__device__ __forceinline__ void gemm_step(const unsigned* addr, f32x4* acc, const bf16x8* bfr) {
  s16x4 lo[32], hi[32];
  frag_issue<KIND, 0>(addr, lo, hi);
  if constexpr (PD > 1) frag_issue<KIND, 1>(addr, lo, hi);
  if constexpr (PD > 2) frag_issue<KIND, 2>(addr, lo, hi);
  if constexpr (PD > 3) frag_issue<KIND, 3>(addr, lo, hi);
  if constexpr (PD > 4) frag_issue<KIND, 4>(addr, lo, hi);
  if constexpr (PD > 5) frag_issue<KIND, 5>(addr, lo, hi);
  if constexpr (PD > 6) frag_issue<KIND, 6>(addr, lo, hi);
  if constexpr (PD > 7) frag_issue<KIND, 7>(addr, lo, hi);
  static_assert(PD <= 7, "lgkmcnt 4 bits");
  gemm_step_rec<KIND, 0>(addr, acc, bfr, lo, hi);
}

__device__ __forceinline__ void glds16(const void* g, char* l) {
  __builtin_amdgcn_global_load_lds((const unsigned*)g, (unsigned*)l, 16, 0, 0);
}

__device__ __forceinline__ void issue_tile(int t, int L, int lane, char* lds, const bf16* wq_h,
                                           const bf16* k_h, const bf16* v_h, const bf16* wo_h) {
  char* slot = lds + (t & (RING - 1)) * TILE;
  if (t < 7) {
    const bf16* src = wq_h + (size_t)(64 * t) * HD;
#pragma unroll
    for (int i = 0; i < IPT; ++i) {
      int ii = L * IPT + i;
      int row = 2 * ii + (lane >> 5), pos = lane & 31;
      int f = (row & 3) | ((row >> 1) & 4);
      int c = pos ^ (f << 1);
      glds16(src + (size_t)row * HD + c * 8, slot + ii * 1024);
    }
  } else if (t < 23) {
    int j = (t - 7) >> 1;
    if (((t - 7) & 1) == 0) {
      const bf16* src = k_h + (size_t)(64 * j) * DH;
#pragma unroll
      for (int i = 0; i < IPT; ++i) {
        int ii = L * IPT + i;
        int row = 2 * ii + (lane >> 5), pos = lane & 31;
        int c = pos ^ (row & 15);
        glds16(src + row * DH + c * 8, slot + ii * 1024);
      }
    } else {
      const bf16* src = v_h + (size_t)(64 * j) * DH;
#pragma unroll
      for (int i = 0; i < IPT; ++i) {
        int ii = L * IPT + i;
        int row = 2 * ii + (lane >> 5), pos = lane & 31;
        int c = pos ^ ((row & 7) << 1);
        glds16(src + row * DH + c * 8, slot + ii * 1024);
      }
    }
  } else {
    const bf16* src = wo_h + 64 * (t - 23);
#pragma unroll
    for (int i = 0; i < IPT; ++i) {
      int ii = L * IPT + i;
      int row = 8 * ii + (lane >> 3), pos = lane & 7;
      int c = pos ^ (((row >> 1) & 3) << 1);
      glds16(src + row * DM + c * 8, slot + ii * 1024);
    }
  }
}

#define STAMP(i) do {} while (0)
__global__ void __launch_bounds__(NTHR, 1)
mha_fused_kernel(Params P) {
  extern __shared__ __attribute__((aligned(16))) char lds[];
  const int bid = blockIdx.x;
  const int xcd = bid & 7, rk = bid >> 3;
  const int h = 2 * xcd + (rk & 1), pb = rk >> 1;
  const int p = pb >> 3, b = pb & 7, u = pb * NH + h;
  const int tid = threadIdx.x, wid = __builtin_amdgcn_readfirstlane(tid >> 6), lane = tid & 63;

  const bf16* wq_h = P.wq[p] + h * DH;
  const bf16* k_h = P.k[p] + (size_t)((b * NH + h) * SK) * DH;
  const bf16* v_h = P.v[p] + (size_t)((b * NH + h) * SK) * DH;
  const bf16* wo_h = P.wo[p] + (size_t)(h * DH) * DM;
  bf16* part = (bf16*)(P.ws + WS_CTR_BYTES);
  unsigned* ctr = (unsigned*)(P.ws + (u >> 4) * 256);

  STAMP(0);
  if (wid >= NCW) {
    const int L = wid - NCW;
    issue_tile(0, L, lane, lds, wq_h, k_h, v_h, wo_h);
    issue_tile(1, L, lane, lds, wq_h, k_h, v_h, wo_h);
    issue_tile(2, L, lane, lds, wq_h, k_h, v_h, wo_h);
    for (int t = 0; t < NSTEP; ++t) {
      if (t <= NSTEP - 3) asm volatile("s_waitcnt vmcnt(%0)" ::"i"(2 * IPT) : "memory");
      else if (t == NSTEP - 2) asm volatile("s_waitcnt vmcnt(%0)" ::"i"(IPT) : "memory");
      else asm volatile("s_waitcnt vmcnt(0)" ::: "memory");
      wg_barrier();
      if (t + 3 < NSTEP) issue_tile(t + 3, L, lane, lds, wq_h, k_h, v_h, wo_h);
    }
  } else {
    __builtin_amdgcn_s_setprio(2);
    const int m16 = lane & 15, g = lane >> 4, q4 = m16 >> 2, p4 = lane & 3;
    const unsigned lbase = (unsigned)(uintptr_t)(__attribute__((address_space(3))) char*)lds;
    const unsigned fl5 = (unsigned)(q4 | ((g & 1) << 2)) << 5;
    const unsigned hw5 = (unsigned)(((g & 1) << 1) | (q4 >> 1)) << 5;
    bf16x8 qf[8];
    {
      const bf16* xr = P.x[p] + (size_t)(b * SQ + 16 * wid + m16) * DM + 8 * g;
      bf16x8 xa[2] = {*(const bf16x8*)(xr), *(const bf16x8*)(xr + 32)};
      f32x4 acc[16];
#pragma unroll
      for (int i = 0; i < 16; ++i) acc[i] = (f32x4){0.f, 0.f, 0.f, 0.f};
#pragma unroll 1
      for (int t = 0; t < 7; ++t) {
        bf16x8 xb[2] = {xa[0], xa[1]};
        if (t < 6) { xa[0] = *(const bf16x8*)(xr + 64 * (t + 1)); xa[1] = *(const bf16x8*)(xr + 64 * (t + 1) + 32); }
        wg_barrier();
        unsigned base = lbase + (t & (RING - 1)) * TILE + (8 * g + q4) * 512 + (p4 << 3);
        unsigned f5 = fl5;
        asm volatile("" : "+v"(f5));
        unsigned addr[8];
#pragma unroll
        for (int c = 0; c < 8; ++c) addr[c] = base + (((unsigned)c << 5) ^ f5);
        gemm_step<0>(addr, acc, xb);
      }
#pragma unroll
      for (int s = 0; s < 8; ++s) qf[s] = pack8(acc[2 * s], acc[2 * s + 1]);
    }

    STAMP(1);
    const float CS = 0.0625f * 1.4426950408889634f;
    float m_run = -3.0e38f, l_run = 0.f;
    f32x4 oacc[16];
#pragma unroll
    for (int i = 0; i < 16; ++i) oacc[i] = (f32x4){0.f, 0.f, 0.f, 0.f};
#pragma unroll 1
    for (int j = 0; j < 8; ++j) {
      wg_barrier();
      bf16x8 pf[2];
      {
        unsigned base = lbase + ((7 + 2 * j) & (RING - 1)) * TILE + m16 * 512 + ((g & 1) << 3);
        unsigned mk = (unsigned)(m16 ^ (g >> 1)) << 4;
        asm volatile("" : "+v"(mk));
        unsigned addr[8];
#pragma unroll
        for (int c = 0; c < 8; ++c) addr[c] = base + (((unsigned)c << 5) ^ mk);
        f32x4 sacc[4];
#pragma unroll
        for (int i = 0; i < 4; ++i) sacc[i] = (f32x4){0.f, 0.f, 0.f, 0.f};
        gemm_step<1>(addr, sacc, qf);
        float mx = sacc[0][0];
#pragma unroll
        for (int kt = 0; kt < 4; ++kt)
#pragma unroll
          for (int r = 0; r < 4; ++r) mx = fmaxf(mx, sacc[kt][r]);
        mx = fmaxf(mx, __shfl_xor(mx, 16));
        mx = fmaxf(mx, __shfl_xor(mx, 32));
        float m_new = fmaxf(m_run, mx);
        float alpha = __builtin_amdgcn_exp2f((m_run - m_new) * CS);
        float mc = m_new * CS;
        float ps = 0.f;
#pragma unroll
        for (int kt = 0; kt < 4; ++kt)
#pragma unroll
          for (int r = 0; r < 4; ++r) {
            float e = __builtin_amdgcn_exp2f(fmaf(sacc[kt][r], CS, -mc));
            sacc[kt][r] = e;
            ps += e;
          }
        l_run = l_run * alpha + ps;
        m_run = m_new;
        if (__any(alpha < 1.f)) {
#pragma unroll
          for (int i = 0; i < 16; ++i) oacc[i] *= alpha;
        }
        pf[0] = pack8(sacc[0], sacc[1]);
        pf[1] = pack8(sacc[2], sacc[3]);
      }
      wg_barrier();
      {
        unsigned base = lbase + ((8 + 2 * j) & (RING - 1)) * TILE + (4 * g + q4) * 512 + (p4 << 3);
        unsigned f5 = fl5;
        asm volatile("" : "+v"(f5));
        unsigned addr[8];
#pragma unroll
        for (int c = 0; c < 8; ++c) addr[c] = base + (((unsigned)c << 5) ^ f5);
        gemm_step<2>(addr, oacc, pf);
      }
    }
    STAMP(2);
    l_run += __shfl_xor(l_run, 16);
    l_run += __shfl_xor(l_run, 32);
    float inv = 1.0f / l_run;
    bf16x8 of[8];
#pragma unroll
    for (int s = 0; s < 8; ++s) of[s] = pack8(oacc[2 * s] * inv, oacc[2 * s + 1] * inv);

    bf16* my_part = part + (size_t)u * PART_UNIT;
    const __amdgpu_buffer_rsrc_t rs = __builtin_amdgcn_make_buffer_rsrc(
        (void*)my_part, 0, (int)(PART_UNIT * 2), 0x00020000);
#pragma unroll 1
    for (int nt = 0; nt < 7; ++nt) {
      wg_barrier();
      unsigned base = lbase + ((23 + nt) & (RING - 1)) * TILE + (4 * g + q4) * 128 + (p4 << 3);
      unsigned h5 = hw5;
      asm volatile("" : "+v"(h5));
      unsigned addr[4];
#pragma unroll
      for (int c = 0; c < 4; ++c) addr[c] = base + (((unsigned)c << 5) ^ h5);
      f32x4 acc[4];
#pragma unroll
      for (int i = 0; i < 4; ++i) acc[i] = (f32x4){0.f, 0.f, 0.f, 0.f};
      gemm_step<3>(addr, acc, of);
#pragma unroll
      for (int ct = 0; ct < 4; ++ct) {
        int off = ((16 * wid + m16) * DM + 64 * nt + 16 * ct + 4 * g) * 2;
        u32x2 w = {cvtpk(acc[ct][0], acc[ct][1]), cvtpk(acc[ct][2], acc[ct][3])};
        __builtin_amdgcn_raw_buffer_store_b64(w, rs, off, 0, 16);
      }
    }
    STAMP(3);
    asm volatile("s_waitcnt vmcnt(0)" ::: "memory");
  }

  wg_barrier();
  if (tid == 0) {
    __hip_atomic_fetch_add(ctr, 1u, __ATOMIC_RELAXED, __HIP_MEMORY_SCOPE_AGENT);
    unsigned spins = 0;
    while (__hip_atomic_load(ctr, __ATOMIC_RELAXED, __HIP_MEMORY_SCOPE_AGENT) < (unsigned)NH) {
      __builtin_amdgcn_s_sleep(2);
      if (++spins > (1u << 22)) break;
    }
    __builtin_amdgcn_fence(__ATOMIC_ACQUIRE, "agent");
    asm volatile("s_waitcnt vmcnt(0)" ::: "memory");
  }
  wg_barrier();
  STAMP(4);
  {
    const bf16* gp = part + (size_t)(u & ~15) * PART_UNIT;
    bf16* outp = P.out + (size_t)(b * SQ) * (2 * DM) + p * DM;
    for (int idx = tid; idx < 9 * (DM / 8); idx += NTHR) {
      int r = idx / (DM / 8), c8 = idx - r * (DM / 8);
      int row = 9 * h + r;
      const bf16* src = gp + (size_t)row * DM + 8 * c8;
      float sacc[8] = {0.f, 0.f, 0.f, 0.f, 0.f, 0.f, 0.f, 0.f};
#pragma unroll
      for (int hh = 0; hh < NH; ++hh) {
        u32x4 w = *(const u32x4*)(src + (size_t)hh * PART_UNIT);
#pragma unroll
        for (int e = 0; e < 4; ++e) {
          sacc[2 * e] += __uint_as_float(w[e] << 16);
          sacc[2 * e + 1] += __uint_as_float(w[e] & 0xffff0000u);
        }
      }
      u32x4 o = {cvtpk(sacc[0], sacc[1]), cvtpk(sacc[2], sacc[3]), cvtpk(sacc[4], sacc[5]), cvtpk(sacc[6], sacc[7])};
      *(u32x4*)(outp + (size_t)row * (2 * DM) + 8 * c8) = o;
    }
  }
  STAMP(5);
}

extern "C" void kernel_launch(void* const* d_in, const int* in_sizes, int n_in,
                              void* d_out, int out_size, void* d_ws, size_t ws_size,
                              hipStream_t stream) {
  Params P;
  for (int p = 0; p < 2; ++p) {
    P.x[p] = (const bf16*)d_in[5 * p + 0];
    P.k[p] = (const bf16*)d_in[5 * p + 1];
    P.v[p] = (const bf16*)d_in[5 * p + 2];
    P.wq[p] = (const bf16*)d_in[5 * p + 3];
    P.wo[p] = (const bf16*)d_in[5 * p + 4];
  }
  P.out = (bf16*)d_out;
  P.ws = (char*)d_ws;
  hipFuncSetAttribute((const void*)mha_fused_kernel, hipFuncAttributeMaxDynamicSharedMemorySize, LDS_BYTES);
  hipMemsetAsync(d_ws, 0, WS_CTR_BYTES, stream);
  hipLaunchKernelGGL(mha_fused_kernel, dim3(256), dim3(NTHR), LDS_BYTES, stream, P);
}
```

```cpp
#include <hip/hip_runtime.h>
#include <hip/hip_bf16.h>
#include <stdint.h>
#include <type_traits>

typedef __attribute__((ext_vector_type(8))) short bf16x8;
typedef __attribute__((ext_vector_type(4))) short s16x4;
typedef __attribute__((ext_vector_type(4))) float f32x4;
typedef __attribute__((ext_vector_type(4))) unsigned u32x4;
typedef __attribute__((ext_vector_type(2))) unsigned u32x2;
typedef __hip_bfloat16 bf16;

constexpr int NB = 8, NH = 16, SQ = 144, SK = 512, DH = 256, DM = 448, HD = NH * DH;
constexpr int QT = 1, NCW = 9, NLW = 1, NW = NCW + NLW, NTHR = NW * 64;
constexpr int TILE = 32768, RING = 4;
constexpr int NSTEP = 30;
constexpr int LDS_BYTES = RING * TILE;
constexpr int IPT = 32 / NLW;
constexpr size_t WS_CTR_BYTES = 16 * 256;
constexpr size_t PART_UNIT = (size_t)SQ * DM;

struct Params {
  const bf16* x[2];
  const bf16* k[2];
  const bf16* v[2];
  const bf16* wq[2];
  const bf16* wo[2];
  bf16* out;
  char* ws;
};

__device__ __forceinline__ unsigned cvtpk(float lo, float hi) {
  unsigned r;
  asm volatile("v_cvt_pk_bf16_f32 %0, %1, %2" : "=v"(r) : "v"(lo), "v"(hi));
  return r;
}
__device__ __forceinline__ bf16x8 pack8(const f32x4& a, const f32x4& b) {
  u32x4 w = {cvtpk(a[0], a[1]), cvtpk(a[2], a[3]), cvtpk(b[0], b[1]), cvtpk(b[2], b[3])};
  return __builtin_bit_cast(bf16x8, w);
}
typedef __attribute__((address_space(3))) s16x4 lds_s16x4;
__device__ __forceinline__ s16x4 lds_tr(unsigned a) {
  return __builtin_amdgcn_ds_read_tr16_b64_v4i16((lds_s16x4*)(uintptr_t)a);
}
__device__ __forceinline__ s16x4 lds_rd64(unsigned a) {
  return *(lds_s16x4*)(uintptr_t)a;
}
__device__ __forceinline__ bf16x8 join(s16x4 a, s16x4 b) {
  return (bf16x8){a[0], a[1], a[2], a[3], b[0], b[1], b[2], b[3]};
}
__device__ __forceinline__ void wg_barrier() {
  asm volatile("s_waitcnt lgkmcnt(0)\n\ts_barrier" ::: "memory");
}

template <int I, int N, class F>
__device__ __forceinline__ void static_for(F&& f) {
  if constexpr (I < N) { f(std::integral_constant<int, I>{}); static_for<I + 1, N>(f); }
}
template <int KIND> struct Cfg;
template <> struct Cfg<0> {
  static constexpr bool TR = true;
  static constexpr int ia(int i) { return i >> 2; }
  static constexpr int olo(int i) { return ((i >> 1) & 1) * 256 + (i & 1) * 32 * 512; }
  static constexpr int ohi(int i) { return olo(i) + 4 * 512; }
  static constexpr int iacc(int i) { return (i >> 2) + 8 * ((i >> 1) & 1); }
  static constexpr int ib(int i) { return i & 1; }
};
template <> struct Cfg<1> {
  static constexpr bool TR = false;
  static constexpr int ia(int i) { return 2 * (i >> 3); }
  static constexpr int olo(int i) { return (i & 3) * 8192 + ((i >> 2) & 1) * 256; }
  static constexpr int ohi(int i) { return olo(i); }
  static constexpr int iacc(int i) { return i & 3; }
  static constexpr int ib(int i) { return (i >> 3) + 4 * ((i >> 2) & 1); }
};
template <> struct Cfg<2> {
  static constexpr bool TR = true;
  static constexpr int ia(int i) { return i >> 2; }
  static constexpr int olo(int i) { return ((i >> 1) & 1) * 256 + (i & 1) * 32 * 512; }
  static constexpr int ohi(int i) { return olo(i) + 16 * 512; }
  static constexpr int iacc(int i) { return (i >> 2) + 8 * ((i >> 1) & 1); }
  static constexpr int ib(int i) { return i & 1; }
};
template <> struct Cfg<3> {
  static constexpr bool TR = true;
  static constexpr int ia(int i) { return i >> 3; }
  static constexpr int olo(int i) { return (i & 7) * 4096; }
  static constexpr int ohi(int i) { return olo(i) + 2048; }
  static constexpr int iacc(int i) { return i >> 3; }
  static constexpr int ib(int i) { return i & 7; }
};

#define PDEPTH 3
constexpr int PD = PDEPTH;
template <int KIND, int I>
__device__ __forceinline__ void frag_issue(const unsigned* addr, s16x4* lo, s16x4* hi) {
  using C = Cfg<KIND>;
  s16x4 a, b;
  if constexpr (C::TR) {
    asm volatile("ds_read_b64_tr_b16 %0, %1 offset:%2" : "=v"(a) : "v"(addr[C::ia(I)]), "i"(C::olo(I)));
    asm volatile("ds_read_b64_tr_b16 %0, %1 offset:%2" : "=v"(b) : "v"(addr[C::ia(I)]), "i"(C::ohi(I)));
  } else {
    asm volatile("ds_read_b64 %0, %1 offset:%2" : "=v"(a) : "v"(addr[C::ia(I)]), "i"(C::olo(I)));
    asm volatile("ds_read_b64 %0, %1 offset:%2" : "=v"(b) : "v"(addr[C::ia(I) + 1]), "i"(C::ohi(I)));
  }
  lo[I] = a; hi[I] = b;
}
template <int KIND, int I, int NA, int NBF>
__device__ __forceinline__ void gemm_step_rec(const unsigned* addr, f32x4 (*acc)[NA], const bf16x8 (*bfr)[NBF],
                                              s16x4* lo, s16x4* hi) {
  using C = Cfg<KIND>;
  if constexpr (I < 32) {
    if constexpr (I + PD < 32) frag_issue<KIND, I + PD>(addr, lo, hi);
    constexpr int pend = 2 * ((I + PD < 32) ? PD : (31 - I));
    s16x4 a = lo[I], b = hi[I];
    asm volatile("s_waitcnt lgkmcnt(%2)" : "+v"(a), "+v"(b) : "i"(pend));
    bf16x8 af = join(a, b);
#pragma unroll
    for (int q = 0; q < QT; ++q)
      acc[q][C::iacc(I)] = __builtin_amdgcn_mfma_f32_16x16x32_bf16(af, bfr[q][C::ib(I)], acc[q][C::iacc(I)], 0, 0, 0);
    __builtin_amdgcn_sched_barrier(0);
    gemm_step_rec<KIND, I + 1, NA, NBF>(addr, acc, bfr, lo, hi);
  }
}
template <int KIND, int NA, int NBF>
__device__ __forceinline__ void gemm_step(const unsigned* addr, f32x4 (*acc)[NA], const bf16x8 (*bfr)[NBF]) {
  s16x4 lo[32], hi[32];
  frag_issue<KIND, 0>(addr, lo, hi);
  frag_issue<KIND, 1>(addr, lo, hi);
  frag_issue<KIND, 2>(addr, lo, hi);
  gemm_step_rec<KIND, 0, NA, NBF>(addr, acc, bfr, lo, hi);
}

__device__ __forceinline__ void glds16(const void* g, char* l) {
  __builtin_amdgcn_global_load_lds((const unsigned*)g, (unsigned*)l, 16, 0, 0);
}

__device__ __forceinline__ void issue_tile(int t, int L, int lane, char* lds, const bf16* wq_h,
                                           const bf16* k_h, const bf16* v_h, const bf16* wo_h) {
  char* slot = lds + (t & (RING - 1)) * TILE;
  if (t < 7) {
    const bf16* src = wq_h + (size_t)(64 * t) * HD;
#pragma unroll 2
    for (int i = 0; i < IPT; ++i) {
      int ii = L * IPT + i;
      int row = 2 * ii + (lane >> 5), pos = lane & 31;
      int f = (row & 3) | ((row >> 1) & 4);
      int c = pos ^ (f << 1);
      glds16(src + (size_t)row * HD + c * 8, slot + ii * 1024);
    }
  } else if (t < 23) {
    int j = (t - 7) >> 1;
    if (((t - 7) & 1) == 0) {
      const bf16* src = k_h + (size_t)(64 * j) * DH;
#pragma unroll 2
      for (int i = 0; i < IPT; ++i) {
        int ii = L * IPT + i;
        int row = 2 * ii + (lane >> 5), pos = lane & 31;
        int c = pos ^ (row & 15);
        glds16(src + row * DH + c * 8, slot + ii * 1024);
      }
    } else {
      const bf16* src = v_h + (size_t)(64 * j) * DH;
#pragma unroll 2
      for (int i = 0; i < IPT; ++i) {
        int ii = L * IPT + i;
        int row = 2 * ii + (lane >> 5), pos = lane & 31;
        int c = pos ^ ((row & 7) << 1);
        glds16(src + row * DH + c * 8, slot + ii * 1024);
      }
    }
  } else {
    const bf16* src = wo_h + 64 * (t - 23);
#pragma unroll 2
    for (int i = 0; i < IPT; ++i) {
      int ii = L * IPT + i;
      int row = 8 * ii + (lane >> 3), pos = lane & 7;
      int c = pos ^ (((row >> 1) & 3) << 1);
      glds16(src + row * DM + c * 8, slot + ii * 1024);
    }
  }
}

#define STAMP(i) do {} while (0)
__global__ void __launch_bounds__(NTHR, 1)
mha_fused_kernel(Params P) {
  extern __shared__ __attribute__((aligned(16))) char lds[];
  const int bid = blockIdx.x;
  const int xcd = bid & 7, rk = bid >> 3;
  const int h = 2 * xcd + (rk & 1), pb = rk >> 1;
  const int p = pb >> 3, b = pb & 7, u = pb * NH + h;
  const int tid = threadIdx.x, wid = __builtin_amdgcn_readfirstlane(tid >> 6), lane = tid & 63;

  const bf16* wq_h = P.wq[p] + h * DH;
  const bf16* k_h = P.k[p] + (size_t)((b * NH + h) * SK) * DH;
  const bf16* v_h = P.v[p] + (size_t)((b * NH + h) * SK) * DH;
  const bf16* wo_h = P.wo[p] + (size_t)(h * DH) * DM;
  bf16* part = (bf16*)(P.ws + WS_CTR_BYTES);
  unsigned* ctr = (unsigned*)(P.ws + (u >> 4) * 256);

  STAMP(0);
  if (wid >= NCW) {
    const int L = 0;
    issue_tile(0, L, lane, lds, wq_h, k_h, v_h, wo_h);
    issue_tile(1, L, lane, lds, wq_h, k_h, v_h, wo_h);
    for (int t = 0; t < NSTEP; ++t) {
      if (t <= NSTEP - 2) asm volatile("s_waitcnt vmcnt(%0)" ::"i"(IPT) : "memory");
      else asm volatile("s_waitcnt vmcnt(0)" ::: "memory");
      wg_barrier();
      if (t + 2 < NSTEP) issue_tile(t + 2, L, lane, lds, wq_h, k_h, v_h, wo_h);
    }
  } else {
    const int m16 = lane & 15, g = lane >> 4, q4 = m16 >> 2, p4 = lane & 3;
    const unsigned lbase = (unsigned)(uintptr_t)(__attribute__((address_space(3))) char*)lds;
    const unsigned fl5 = (unsigned)(q4 | ((g & 1) << 2)) << 5;
    const unsigned hw5 = (unsigned)(((g & 1) << 1) | (q4 >> 1)) << 5;
    const int row0 = 16 * QT * wid + m16;
    bf16x8 qf[QT][8];
    {
      const bf16* xr = P.x[p] + (size_t)(b * SQ + row0) * DM + 8 * g;
      f32x4 acc[QT][16];
#pragma unroll
      for (int q = 0; q < QT; ++q)
#pragma unroll
        for (int i = 0; i < 16; ++i) acc[q][i] = (f32x4){0.f, 0.f, 0.f, 0.f};
#pragma unroll 1
      for (int t = 0; t < 7; ++t) {
        bf16x8 xb[QT][2];
#pragma unroll
        for (int q = 0; q < QT; ++q) {
          xb[q][0] = *(const bf16x8*)(xr + q * 16 * DM + 64 * t);
          xb[q][1] = *(const bf16x8*)(xr + q * 16 * DM + 64 * t + 32);
        }
        wg_barrier();
        unsigned base = lbase + (t & (RING - 1)) * TILE + (8 * g + q4) * 512 + (p4 << 3);
        unsigned f5 = fl5; asm volatile("" : "+v"(f5));
        unsigned addr[8];
#pragma unroll
        for (int c = 0; c < 8; ++c) addr[c] = base + (((unsigned)c << 5) ^ f5);
        gemm_step<0>(addr, acc, xb);
      }
#pragma unroll
      for (int q = 0; q < QT; ++q)
#pragma unroll
        for (int s = 0; s < 8; ++s) qf[q][s] = pack8(acc[q][2 * s], acc[q][2 * s + 1]);
    }
    STAMP(1);
    const float CS = 0.0625f * 1.4426950408889634f;
    float m_run[QT], l_run[QT];
    f32x4 oacc[QT][16];
#pragma unroll
    for (int q = 0; q < QT; ++q) {
      m_run[q] = -3.0e38f; l_run[q] = 0.f;
#pragma unroll
      for (int i = 0; i < 16; ++i) oacc[q][i] = (f32x4){0.f, 0.f, 0.f, 0.f};
    }
#pragma unroll 1
    for (int j = 0; j < 8; ++j) {
      wg_barrier();
      bf16x8 pf[QT][2];
      {
        unsigned base = lbase + ((7 + 2 * j) & (RING - 1)) * TILE + m16 * 512 + ((g & 1) << 3);
        unsigned mk = (unsigned)(m16 ^ (g >> 1)) << 4; asm volatile("" : "+v"(mk));
        unsigned addr[8];
#pragma unroll
        for (int c = 0; c < 8; ++c) addr[c] = base + (((unsigned)c << 5) ^ mk);
        f32x4 sacc[QT][4];
#pragma unroll
        for (int q = 0; q < QT; ++q)
#pragma unroll
          for (int i = 0; i < 4; ++i) sacc[q][i] = (f32x4){0.f, 0.f, 0.f, 0.f};
        gemm_step<1>(addr, sacc, qf);
#pragma unroll
        for (int q = 0; q < QT; ++q) {
          float mx = sacc[q][0][0];
#pragma unroll
          for (int kt = 0; kt < 4; ++kt)
#pragma unroll
            for (int r = 0; r < 4; ++r) mx = fmaxf(mx, sacc[q][kt][r]);
          mx = fmaxf(mx, __shfl_xor(mx, 16));
          mx = fmaxf(mx, __shfl_xor(mx, 32));
          float m_new = fmaxf(m_run[q], mx);
          float alpha = __builtin_amdgcn_exp2f((m_run[q] - m_new) * CS);
          float mc = m_new * CS, ps = 0.f;
#pragma unroll
          for (int kt = 0; kt < 4; ++kt)
#pragma unroll
            for (int r = 0; r < 4; ++r) {
              float e = __builtin_amdgcn_exp2f(fmaf(sacc[q][kt][r], CS, -mc));
              sacc[q][kt][r] = e; ps += e;
            }
          l_run[q] = l_run[q] * alpha + ps;
          m_run[q] = m_new;
          if (__any(alpha < 1.f)) {
#pragma unroll
            for (int i = 0; i < 16; ++i) oacc[q][i] *= alpha;
          }
          pf[q][0] = pack8(sacc[q][0], sacc[q][1]);
          pf[q][1] = pack8(sacc[q][2], sacc[q][3]);
        }
      }
      wg_barrier();
      {
        unsigned base = lbase + ((8 + 2 * j) & (RING - 1)) * TILE + (4 * g + q4) * 512 + (p4 << 3);
        unsigned f5 = fl5; asm volatile("" : "+v"(f5));
        unsigned addr[8];
#pragma unroll
        for (int c = 0; c < 8; ++c) addr[c] = base + (((unsigned)c << 5) ^ f5);
        gemm_step<2>(addr, oacc, pf);
      }
    }
    STAMP(2);
    bf16x8 of[QT][8];
#pragma unroll
    for (int q = 0; q < QT; ++q) {
      float l = l_run[q];
      l += __shfl_xor(l, 16);
      l += __shfl_xor(l, 32);
      float inv = 1.0f / l;
#pragma unroll
      for (int s = 0; s < 8; ++s) of[q][s] = pack8(oacc[q][2 * s] * inv, oacc[q][2 * s + 1] * inv);
    }
    bf16* my_part = part + (size_t)u * PART_UNIT;
    const __amdgpu_buffer_rsrc_t rs = __builtin_amdgcn_make_buffer_rsrc(
        (void*)my_part, 0, (int)(PART_UNIT * 2), 0x00020000);
#pragma unroll 1
    for (int nt = 0; nt < 7; ++nt) {
      wg_barrier();
      unsigned base = lbase + ((23 + nt) & (RING - 1)) * TILE + (4 * g + q4) * 128 + (p4 << 3);
      unsigned h5 = hw5; asm volatile("" : "+v"(h5));
      unsigned addr[4];
#pragma unroll
      for (int c = 0; c < 4; ++c) addr[c] = base + (((unsigned)c << 5) ^ h5);
      f32x4 acc[QT][4];
#pragma unroll
      for (int q = 0; q < QT; ++q)
#pragma unroll
        for (int i = 0; i < 4; ++i) acc[q][i] = (f32x4){0.f, 0.f, 0.f, 0.f};
      gemm_step<3>(addr, acc, of);
#pragma unroll
      for (int q = 0; q < QT; ++q)
#pragma unroll
        for (int ct = 0; ct < 4; ++ct) {
          int off = ((row0 + 16 * q) * DM + 64 * nt + 16 * ct + 4 * g) * 2;
          u32x2 w = {cvtpk(acc[q][ct][0], acc[q][ct][1]), cvtpk(acc[q][ct][2], acc[q][ct][3])};
          __builtin_amdgcn_raw_buffer_store_b64(w, rs, off, 0, 16);
        }
    }
    STAMP(3);
    asm volatile("s_waitcnt vmcnt(0)" ::: "memory");
  }

  wg_barrier();
  if (tid == 0) {
    __hip_atomic_fetch_add(ctr, 1u, __ATOMIC_RELAXED, __HIP_MEMORY_SCOPE_AGENT);
    unsigned spins = 0;
    while (__hip_atomic_load(ctr, __ATOMIC_RELAXED, __HIP_MEMORY_SCOPE_AGENT) < (unsigned)NH) {
      __builtin_amdgcn_s_sleep(2);
      if (++spins > (1u << 22)) break;
    }
    __builtin_amdgcn_fence(__ATOMIC_ACQUIRE, "agent");
    asm volatile("s_waitcnt vmcnt(0)" ::: "memory");
  }
  wg_barrier();
  STAMP(4);
  {
    const bf16* gp = part + (size_t)(u & ~15) * PART_UNIT;
    bf16* outp = P.out + (size_t)(b * SQ) * (2 * DM) + p * DM;
    for (int idx = tid; idx < 9 * (DM / 8); idx += NTHR) {
      int r = idx / (DM / 8), c8 = idx - r * (DM / 8);
      int row = 9 * h + r;
      const bf16* src = gp + (size_t)row * DM + 8 * c8;
      float sacc[8] = {0.f, 0.f, 0.f, 0.f, 0.f, 0.f, 0.f, 0.f};
#pragma unroll
      for (int hh = 0; hh < NH; ++hh) {
        u32x4 w = *(const u32x4*)(src + (size_t)hh * PART_UNIT);
#pragma unroll
        for (int e = 0; e < 4; ++e) {
          sacc[2 * e] += __uint_as_float(w[e] << 16);
          sacc[2 * e + 1] += __uint_as_float(w[e] & 0xffff0000u);
        }
      }
      u32x4 o = {cvtpk(sacc[0], sacc[1]), cvtpk(sacc[2], sacc[3]), cvtpk(sacc[4], sacc[5]), cvtpk(sacc[6], sacc[7])};
      *(u32x4*)(outp + (size_t)row * (2 * DM) + 8 * c8) = o;
    }
  }
  STAMP(5);
}

extern "C" void kernel_launch(void* const* d_in, const int* in_sizes, int n_in,
                              void* d_out, int out_size, void* d_ws, size_t ws_size,
                              hipStream_t stream) {
  Params P;
  for (int p = 0; p < 2; ++p) {
    P.x[p] = (const bf16*)d_in[5 * p + 0];
    P.k[p] = (const bf16*)d_in[5 * p + 1];
    P.v[p] = (const bf16*)d_in[5 * p + 2];
    P.wq[p] = (const bf16*)d_in[5 * p + 3];
    P.wo[p] = (const bf16*)d_in[5 * p + 4];
  }
  P.out = (bf16*)d_out;
  P.ws = (char*)d_ws;
  hipFuncSetAttribute((const void*)mha_fused_kernel, hipFuncAttributeMaxDynamicSharedMemorySize, LDS_BYTES);
  hipMemsetAsync(d_ws, 0, WS_CTR_BYTES, stream);
  hipLaunchKernelGGL(mha_fused_kernel, dim3(256), dim3(NTHR), LDS_BYTES, stream, P);
}
```
